```python
import torch

def _softmax(x, axis=-1):
    return torch.softmax(x, dim=axis)

def _gelu(x):
    return torch.nn.functional.gelu(x, approximate='tanh')

def _silu(x):
    return torch.nn.functional.silu(x)

def _sigmoid(x):
    return torch.sigmoid(x)

def _layernorm(x, axis=-1, eps=1e-05):
    m = x.mean(dim=axis, keepdim=True)
    v = x.var(dim=axis, keepdim=True, unbiased=False)
    return (x - m) / torch.sqrt(v + eps)

def _scatter_add(tgt, idx, src, axis=0):
    return tgt.clone().index_add_(axis, idx, src)

def _segment_sum(src, seg, n):
    out = torch.zeros((n,) + src.shape[1:], dtype=src.dtype)
    return out.index_add_(0, seg, src)

def _pad(x, pw):
    flat = tuple((v for pair in reversed(pw) for v in pair))
    return torch.nn.functional.pad(x, flat)
kernel_name = 'adaLN_cond__spectral_gfnet__dense_activation_silu__prompt'

def setup_inputs():

    _w = lambda a: (lambda t: t.to(torch.bfloat16) if t.is_floating_point() else t)(torch.as_tensor(a))

    def _A_setup():
        B, S, D = (1, 2048, 576)
        Df = D // 2 + 1
        return {'x': _w((lambda *s: torch.randn(*s, dtype=torch.bfloat16) * 0.05)(B, S, D)), 'w_r': _w((lambda *s: torch.randn(*s, dtype=torch.bfloat16) * 1.6)(S, Df) * 0.02), 'w_i': _w((lambda *s: torch.randn(*s, dtype=torch.bfloat16) * 1.6)(S, Df) * 0.02)}
    _A_in = _A_setup()

    def _B_setup():
        B, S, D, F = (1, 2048, 576, 4608)
        return {'x': _w((lambda *s: torch.randn(*s, dtype=torch.bfloat16) * 0.05)(B, S, D)), 'w1': _w((lambda *s: torch.randn(*s, dtype=torch.bfloat16) * 0.8)(D, F) * D ** (-0.5)), 'w2': _w((lambda *s: torch.randn(*s, dtype=torch.bfloat16) * 0.2)(F, D) * F ** (-0.5))}
    _B_in = _B_setup()
    return {'A_x': _A_in['x'], 'A_w_r': _A_in['w_r'], 'A_w_i': _A_in['w_i'], 'B_x': _B_in['x'], 'B_w1': _B_in['w1'], 'B_w2': _B_in['w2'], 'c': _w((lambda *s: torch.randn(*s, dtype=torch.bfloat16) * 0.2)(1, 576) * 8.0), 'w_mod': _w((lambda *s: torch.randn(*s, dtype=torch.bfloat16) * 0.2)(576, 6 * 576) * 576 ** (-0.5) * 8.0)}

def reference(A_x, A_w_r, A_w_i, B_x, B_w1, B_w2, c, w_mod):

    def _A_ref(x, w_r, w_i):
        B, S, D = x.shape
        X = (lambda x, axes=(-2, -1): torch.fft.rfft2(x.float(), dim=axes))(x, axes=(-2, -1))
        W = w_r + 1j * w_i
        X = X * W
        y = (lambda x, s, axes=(-2, -1): torch.fft.irfft2(x, s=s, dim=axes).to(torch.bfloat16))(X, (S, D), axes=(-2, -1))
        return y

    def _B_ref(x, w1, w2):
        h = torch.einsum('bsd,df->bsf', x, w1)
        h = _silu(h)
        out = torch.einsum('bsf,fd->bsd', h, w2)
        return out
    x = A_x
    mod = (_silu(c) @ w_mod).reshape(c.shape[0], 6, 1, -1)
    sa, ca, ga, sb, cb, gb = (mod[:, 0].clone(), mod[:, 1].clone(), mod[:, 2].clone(), mod[:, 3].clone(), mod[:, 4].clone(), mod[:, 5].clone())
    h = _layernorm(x, axis=-1) * (1 + ca) + sa
    x = x + ga * _A_ref(h, A_w_r, A_w_i)
    h = _layernorm(x, axis=-1) * (1 + cb) + sb
    x = x + gb * _B_ref(h, B_w1, B_w2)
    return x
```

```cpp
#include <hip/hip_runtime.h>
#include <hip/hip_bf16.h>

typedef __hip_bfloat16 bf16;
#define S_ 2048
#define D_ 576
#define DF_ 289
#define F_ 4608

__device__ __forceinline__ float bf2f(bf16 v) { return __bfloat162float(v); }
__device__ __forceinline__ bf16 f2bf(float v) { return __float2bfloat16(v); }
__device__ __forceinline__ float rbf(float v) { return bf2f(f2bf(v)); }

__global__ void k_mod(const bf16* c, const bf16* wmod, float* mod) {
    int j = blockIdx.x * blockDim.x + threadIdx.x;
    if (j >= 6 * D_) return;
    float acc = 0.f;
    for (int i = 0; i < D_; ++i) {
        float cv = bf2f(c[i]);
        float s = rbf(cv / (1.f + __expf(-cv)));
        acc += s * bf2f(wmod[(size_t)i * 6 * D_ + j]);
    }
    mod[j] = rbf(acc);
}

__device__ float block_sum(float v, float* sh) {
    int t = threadIdx.x;
    sh[t] = v; __syncthreads();
    for (int o = blockDim.x / 2; o > 0; o >>= 1) { if (t < o) sh[t] += sh[t + o]; __syncthreads(); }
    float r = sh[0]; __syncthreads();
    return r;
}

__global__ void k_ln(const float* xin, const float* shift, const float* scale, float* hout) {
    __shared__ float sh[256];
    int row = blockIdx.x, t = threadIdx.x;
    float v[3];
    float s = 0.f;
    for (int i = 0; i < 3; ++i) { int d = t + 256 * i; v[i] = (d < D_) ? xin[(size_t)row * D_ + d] : 0.f; s += v[i]; }
    float mean = block_sum(s, sh) / D_;
    float q = 0.f;
    for (int i = 0; i < 3; ++i) { int d = t + 256 * i; if (d < D_) { float e = v[i] - mean; q += e * e; } }
    float var = block_sum(q, sh) / D_;
    float mb = rbf(mean), vb = rbf(var);
    float den = rbf(sqrtf(rbf(vb + 1e-5f)));
    for (int i = 0; i < 3; ++i) {
        int d = t + 256 * i;
        if (d < D_) {
            float xm = rbf(v[i] - mb);
            float n = rbf(xm / den);
            float sc = rbf(1.f + scale[d]);
            float h = rbf(n * sc);
            h = rbf(h + shift[d]);
            hout[(size_t)row * D_ + d] = h;
        }
    }
}

__global__ void k_cvt(const bf16* x, float* xf, int n) {
    int i = blockIdx.x * blockDim.x + threadIdx.x;
    if (i < n) xf[i] = bf2f(x[i]);
}

__global__ void k_rdft(const float* h, float2* Z1) {
    int s = blockIdx.x;
    __shared__ float row[D_];
    for (int d = threadIdx.x; d < D_; d += blockDim.x) row[d] = h[(size_t)s * D_ + d];
    __syncthreads();
    for (int k = threadIdx.x; k < DF_; k += blockDim.x) {
        float re = 0.f, im = 0.f;
        for (int d = 0; d < D_; ++d) {
            int m = (k * d) % D_;
            float sn, cs; sincospif(-2.f * m / D_, &sn, &cs);
            re += row[d] * cs; im += row[d] * sn;
        }
        Z1[(size_t)s * DF_ + k] = make_float2(re, im);
    }
}

__global__ void k_cdft(const float2* Z1, float2* X, const bf16* wr, const bf16* wi, int inverse) {
    int k = blockIdx.x;
    __shared__ float2 col[S_];
    for (int s = threadIdx.x; s < S_; s += blockDim.x) col[s] = Z1[(size_t)s * DF_ + k];
    __syncthreads();
    float sg = inverse ? 2.f : -2.f;
    for (int sp = threadIdx.x; sp < S_; sp += blockDim.x) {
        float re = 0.f, im = 0.f;
        for (int s = 0; s < S_; ++s) {
            int m = (s * sp) & (S_ - 1);
            float sn, cs; sincospif(sg * m / S_, &sn, &cs);
            re += col[s].x * cs - col[s].y * sn;
            im += col[s].x * sn + col[s].y * cs;
        }
        if (!inverse) {
            float a = bf2f(wr[(size_t)sp * DF_ + k]), b = bf2f(wi[(size_t)sp * DF_ + k]);
            float r2 = re * a - im * b, i2 = re * b + im * a;
            re = r2; im = i2;
        }
        X[(size_t)sp * DF_ + k] = make_float2(re, im);
    }
}

__global__ void k_irdft(const float2* U, const float* xf, const float* ga, float* x1) {
    int s = blockIdx.x;
    __shared__ float2 row[DF_];
    for (int k = threadIdx.x; k < DF_; k += blockDim.x) row[k] = U[(size_t)s * DF_ + k];
    __syncthreads();
    for (int d = threadIdx.x; d < D_; d += blockDim.x) {
        float acc = row[0].x + ((d & 1) ? -row[288].x : row[288].x);
        for (int k = 1; k < 288; ++k) {
            int m = (k * d) % D_;
            float sn, cs; sincospif(2.f * m / D_, &sn, &cs);
            acc += 2.f * (row[k].x * cs - row[k].y * sn);
        }
        float y = rbf(acc / ((float)S_ * D_));
        float t = rbf(ga[d] * y);
        x1[(size_t)s * D_ + d] = rbf(xf[(size_t)s * D_ + d] + t);
    }
}

__global__ void k_gemm1(const float* A, const bf16* B, float* hid) {
    int n = blockIdx.x * blockDim.x + threadIdx.x;
    int m = blockIdx.y;
    if (n >= F_) return;
    float acc = 0.f;
    for (int k = 0; k < D_; ++k) acc += A[(size_t)m * D_ + k] * bf2f(B[(size_t)k * F_ + n]);
    float hv = rbf(acc);
    hid[(size_t)m * F_ + n] = rbf(hv / (1.f + __expf(-hv)));
}
__global__ void k_gemm2(const float* hid, const bf16* B, const float* x1, const float* gb, bf16* out) {
    int n = blockIdx.x * blockDim.x + threadIdx.x;
    int m = blockIdx.y;
    if (n >= D_) return;
    float acc = 0.f;
    for (int k = 0; k < F_; ++k) acc += hid[(size_t)m * F_ + k] * bf2f(B[(size_t)k * D_ + n]);
    float o = rbf(acc);
    float t = rbf(gb[n] * o);
    out[(size_t)m * D_ + n] = f2bf(x1[(size_t)m * D_ + n] + t);
}

extern "C" void kernel_launch(void* const* d_in, const int* in_sizes, int n_in,
                              void* d_out, int out_size, void* d_ws, size_t ws_size,
                              hipStream_t stream) {
    const bf16* x = (const bf16*)d_in[0];
    const bf16* wr = (const bf16*)d_in[1];
    const bf16* wi = (const bf16*)d_in[2];
    const bf16* w1 = (const bf16*)d_in[4];
    const bf16* w2 = (const bf16*)d_in[5];
    const bf16* c = (const bf16*)d_in[6];
    const bf16* wmod = (const bf16*)d_in[7];
    bf16* out = (bf16*)d_out;
    char* ws = (char*)d_ws;
    float* mod = (float*)ws; ws += 6 * D_ * 4 + 256;
    float* xf = (float*)ws; ws += (size_t)S_ * D_ * 4;
    float* h = (float*)ws; ws += (size_t)S_ * D_ * 4;
    float* x1 = (float*)ws; ws += (size_t)S_ * D_ * 4;
    float2* Z1 = (float2*)ws; ws += (size_t)S_ * DF_ * 8;
    float2* X = (float2*)ws; ws += (size_t)S_ * DF_ * 8;
    float* hid = (float*)ws; ws += (size_t)S_ * F_ * 4;

    k_mod<<<(6 * D_ + 255) / 256, 256, 0, stream>>>(c, wmod, mod);
    k_cvt<<<(S_ * D_ + 255) / 256, 256, 0, stream>>>(x, xf, S_ * D_);
    k_ln<<<S_, 256, 0, stream>>>(xf, mod + 0 * D_, mod + 1 * D_, h);
    k_rdft<<<S_, 256, 0, stream>>>(h, Z1);
    k_cdft<<<DF_, 256, 0, stream>>>(Z1, X, wr, wi, 0);
    k_cdft<<<DF_, 256, 0, stream>>>(X, Z1, wr, wi, 1);
    k_irdft<<<S_, 256, 0, stream>>>(Z1, xf, mod + 2 * D_, x1);
    k_ln<<<S_, 256, 0, stream>>>(x1, mod + 3 * D_, mod + 4 * D_, h);
    k_gemm1<<<dim3((F_ + 255) / 256, S_), 256, 0, stream>>>(h, w1, hid);
    k_gemm2<<<dim3((D_ + 255) / 256, S_), 256, 0, stream>>>(hid, w2, x1, mod + 5 * D_, out);
}
```

```cpp
#include <hip/hip_runtime.h>
#include <hip/hip_bf16.h>

typedef __hip_bfloat16 bf16;
#define S_ 2048
#define D_ 576
#define DF_ 289
#define F_ 4608

__device__ __forceinline__ float bf2f(bf16 v) { return __bfloat162float(v); }
__device__ __forceinline__ bf16 f2bf(float v) { return __float2bfloat16(v); }
__device__ __forceinline__ float rbf(float v) { return bf2f(f2bf(v)); }
__device__ __forceinline__ float us2f(unsigned short u) { return __uint_as_float(((unsigned)u) << 16); }

typedef float2 cf;
__device__ __forceinline__ cf cadd(cf a, cf b) { return make_float2(a.x + b.x, a.y + b.y); }
__device__ __forceinline__ cf csub(cf a, cf b) { return make_float2(a.x - b.x, a.y - b.y); }
__device__ __forceinline__ cf cmul(cf a, cf b) { return make_float2(a.x * b.x - a.y * b.y, a.x * b.y + a.y * b.x); }
__device__ __forceinline__ cf cmulc(cf a, cf b) {
    return make_float2(a.x * b.x + a.y * b.y, a.y * b.x - a.x * b.y);
}
template <int DIR> __device__ __forceinline__ cf mulj(cf a) {
    return (DIR < 0) ? make_float2(a.y, -a.x) : make_float2(-a.y, a.x);
}
template <int DIR> __device__ __forceinline__ cf twmul(cf a, cf w) {
    return (DIR < 0) ? cmul(a, w) : cmulc(a, w);
}

template <int DIR> __device__ __forceinline__ void dft4(cf& v0, cf& v1, cf& v2, cf& v3) {
    cf t0 = cadd(v0, v2), t1 = csub(v0, v2), t2 = cadd(v1, v3), t3 = mulj<DIR>(csub(v1, v3));
    v0 = cadd(t0, t2); v2 = csub(t0, t2); v1 = cadd(t1, t3); v3 = csub(t1, t3);
}
template <int DIR> __device__ __forceinline__ void dft8(cf* v) {
    dft4<DIR>(v[0], v[2], v[4], v[6]);
    dft4<DIR>(v[1], v[3], v[5], v[7]);
    const float h = 0.70710678118654752440f;
    cf o1 = v[3], o3 = v[7];
    cf a1 = (DIR < 0) ? make_float2(h * (o1.x + o1.y), h * (o1.y - o1.x)) : make_float2(h * (o1.x - o1.y), h * (o1.y + o1.x));
    cf a2 = mulj<DIR>(v[5]);
    cf a3 = (DIR < 0) ? make_float2(h * (o3.y - o3.x), h * (-o3.x - o3.y)) : make_float2(h * (-o3.x - o3.y), h * (o3.x - o3.y));
    cf e0 = v[0], e1 = v[2], e2 = v[4], e3 = v[6], o0 = v[1];
    v[0] = cadd(e0, o0); v[4] = csub(e0, o0);
    v[1] = cadd(e1, a1); v[5] = csub(e1, a1);
    v[2] = cadd(e2, a2); v[6] = csub(e2, a2);
    v[3] = cadd(e3, a3); v[7] = csub(e3, a3);
}
template <int DIR> __device__ __forceinline__ void dft3(cf& v0, cf& v1, cf& v2) {
    const float c = -0.5f, s = 0.86602540378443864676f * DIR;
    cf t1 = cadd(v1, v2), t2 = csub(v1, v2);
    cf m = make_float2(v0.x + c * t1.x, v0.y + c * t1.y);
    cf r = make_float2(-s * t2.y, s * t2.x);
    v0 = cadd(v0, t1);
    v1 = cadd(m, r);
    v2 = csub(m, r);
}
template <int DIR> __device__ __forceinline__ void dft9(cf* v) {
    dft3<DIR>(v[0], v[3], v[6]);
    dft3<DIR>(v[1], v[4], v[7]);
    dft3<DIR>(v[2], v[5], v[8]);
    const float c1 = 0.76604444311897803520f, s1 = 0.64278760968653932632f * DIR;
    const float c2 = 0.17364817766693034885f, s2 = 0.98480775301220805937f * DIR;
    const float c4 = -0.93969262078590838405f, s4 = 0.34202014332566873304f * DIR;
    v[4] = cmul(v[4], make_float2(c1, s1));
    v[7] = cmul(v[7], make_float2(c2, s2));
    v[5] = cmul(v[5], make_float2(c2, s2));
    v[8] = cmul(v[8], make_float2(c4, s4));
    dft3<DIR>(v[0], v[1], v[2]);
    dft3<DIR>(v[3], v[4], v[5]);
    dft3<DIR>(v[6], v[7], v[8]);
    cf t;
    t = v[1]; v[1] = v[3]; v[3] = t;
    t = v[2]; v[2] = v[6]; v[6] = t;
    t = v[5]; v[5] = v[7]; v[7] = t;
}

__device__ __forceinline__ float wave_sum(float v) {
#pragma unroll
    for (int o = 32; o > 0; o >>= 1) v += __shfl_xor(v, o, 64);
    return v;
}

__global__ __launch_bounds__(256) void k_mod(const bf16* __restrict__ c, const bf16* __restrict__ wmod,
                                             float* __restrict__ mod, float2* __restrict__ tw2048) {
    int b = blockIdx.x, t = threadIdx.x;
    if (b >= 108) {
        int m = (b - 108) * 256 + t;
        float sn, cs; sincospif(-2.f * (float)m / 2048.f, &sn, &cs);
        tw2048[m] = make_float2(cs, sn);
        return;
    }
    __shared__ float red[64][33];
    int cg = t & 3, rg = t >> 2;
    float acc[8];
#pragma unroll
    for (int i = 0; i < 8; ++i) acc[i] = 0.f;
#pragma unroll
    for (int i = 0; i < 9; ++i) {
        int r = rg + 64 * i;
        float cv = bf2f(c[r]);
        float sv = rbf(cv / (1.f + __expf(-cv)));
        uint4 q = *reinterpret_cast<const uint4*>(wmod + (size_t)r * (6 * D_) + b * 32 + cg * 8);
        unsigned u[4] = {q.x, q.y, q.z, q.w};
#pragma unroll
        for (int j = 0; j < 4; ++j) {
            acc[2 * j] += sv * __uint_as_float(u[j] << 16);
            acc[2 * j + 1] += sv * __uint_as_float(u[j] & 0xffff0000u);
        }
    }
#pragma unroll
    for (int i = 0; i < 8; ++i) red[rg][cg * 8 + i] = acc[i];
    __syncthreads();
    if (t < 32) {
        float s = 0.f;
        for (int r = 0; r < 64; ++r) s += red[r][t];
        mod[b * 32 + t] = rbf(s);
    }
}

__device__ __forceinline__ void ln_stats(const float* v, float& mb, float& den) {
    float s = 0.f;
#pragma unroll
    for (int r = 0; r < 9; ++r) s += v[r];
    float mean = wave_sum(s) * (1.f / D_);
    float q = 0.f;
#pragma unroll
    for (int r = 0; r < 9; ++r) { float e = v[r] - mean; q += e * e; }
    float var = wave_sum(q) * (1.f / D_);
    mb = rbf(mean);
    den = rbf(sqrtf(rbf(rbf(var) + 1e-5f)));
}
__device__ __forceinline__ float ln_apply(float x, float mb, float den, float sc1, float sh) {
    float xm = rbf(x - mb);
    float n = rbf(xm / den);
    return rbf(rbf(n * sc1) + sh);
}

__global__ __launch_bounds__(320) void k_pass1(const bf16* __restrict__ x, const float* __restrict__ mod,
                                               float2* __restrict__ Z1T) {
    __shared__ float2 buf0[4][D_];
    __shared__ float2 buf1[4][D_];
    __shared__ float2 tw[D_];
    int tid = threadIdx.x;
    int s0 = blockIdx.x * 8;
    for (int m = tid; m < D_; m += 320) {
        float sn, cs; sincospif(-2.f * (float)m / (float)D_, &sn, &cs);
        tw[m] = make_float2(cs, sn);
    }
    if (tid < 256) {
        int w = tid >> 6, j = tid & 63;
        const bf16* xa = x + (size_t)(s0 + 2 * w) * D_;
        const bf16* xb = xa + D_;
        float va[9], vb[9];
#pragma unroll
        for (int r = 0; r < 9; ++r) { va[r] = bf2f(xa[j + 64 * r]); vb[r] = bf2f(xb[j + 64 * r]); }
        float mba, dena, mbb, denb;
        ln_stats(va, mba, dena);
        ln_stats(vb, mbb, denb);
        cf v[9];
#pragma unroll
        for (int r = 0; r < 9; ++r) {
            int n = j + 64 * r;
            float sh = mod[n], sc1 = rbf(1.f + mod[D_ + n]);
            v[r] = make_float2(ln_apply(va[r], mba, dena, sc1, sh), ln_apply(vb[r], mbb, denb, sc1, sh));
        }
        dft9<-1>(v);
#pragma unroll
        for (int r = 0; r < 9; ++r) buf0[w][9 * j + r] = v[r];
    }
    __syncthreads();
    int w, t;
    if (tid < 256) { w = tid >> 6; t = tid & 63; } else { w = (tid - 256) >> 3; t = 64 + ((tid - 256) & 7); }
    bool act = tid < 288;
    if (act) {
        cf v[8];
        int k = t % 9;
#pragma unroll
        for (int r = 0; r < 8; ++r) v[r] = buf0[w][t + 72 * r];
#pragma unroll
        for (int r = 1; r < 8; ++r) v[r] = cmul(v[r], tw[r * k * 8]);
        dft8<-1>(v);
        int o = (t / 9) * 72 + k;
#pragma unroll
        for (int r = 0; r < 8; ++r) buf1[w][o + 9 * r] = v[r];
    }
    __syncthreads();
    if (act) {
        cf v[8];
#pragma unroll
        for (int r = 0; r < 8; ++r) v[r] = buf1[w][t + 72 * r];
#pragma unroll
        for (int r = 1; r < 8; ++r) v[r] = cmul(v[r], tw[r * t]);
        dft8<-1>(v);
#pragma unroll
        for (int r = 0; r < 8; ++r) buf0[w][t + 72 * r] = v[r];
    }
    __syncthreads();
    for (int task = tid; task < DF_ * 4; task += 320) {
        int p = task & 3, k = task >> 2;
        cf zk = buf0[p][k];
        cf zm = buf0[p][(D_ - k) % D_];
        float4 o;
        o.x = 0.5f * (zk.x + zm.x);
        o.y = 0.5f * (zk.y - zm.y);
        o.z = 0.5f * (zk.y + zm.y);
        o.w = -0.5f * (zk.x - zm.x);
        *reinterpret_cast<float4*>(Z1T + (size_t)k * S_ + s0 + 2 * p) = o;
    }
}

template <int DIR>
__device__ __forceinline__ void fft2048_regs(cf* v, float2* bufA, float2* bufB, const float2* tw, int j) {
    dft4<DIR>(v[0], v[2], v[4], v[6]);
    dft4<DIR>(v[1], v[3], v[5], v[7]);
#pragma unroll
    for (int r = 0; r < 4; ++r) { bufA[4 * j + r] = v[2 * r]; bufA[4 * (j + 256) + r] = v[2 * r + 1]; }
    __syncthreads();
    {
        int k = j & 3;
#pragma unroll
        for (int r = 0; r < 8; ++r) v[r] = bufA[j + 256 * r];
#pragma unroll
        for (int r = 1; r < 8; ++r) v[r] = twmul<DIR>(v[r], tw[r * k * 64]);
        dft8<DIR>(v);
        int o = (j >> 2) * 32 + k;
#pragma unroll
        for (int r = 0; r < 8; ++r) bufB[o + 4 * r] = v[r];
    }
    __syncthreads();
    {
        int k = j & 31;
#pragma unroll
        for (int r = 0; r < 8; ++r) v[r] = bufB[j + 256 * r];
#pragma unroll
        for (int r = 1; r < 8; ++r) v[r] = twmul<DIR>(v[r], tw[r * k * 8]);
        dft8<DIR>(v);
        int o = (j >> 5) * 256 + k;
#pragma unroll
        for (int r = 0; r < 8; ++r) bufA[o + 32 * r] = v[r];
    }
    __syncthreads();
    {
#pragma unroll
        for (int r = 0; r < 8; ++r) v[r] = bufA[j + 256 * r];
#pragma unroll
        for (int r = 1; r < 8; ++r) v[r] = twmul<DIR>(v[r], tw[r * j]);
        dft8<DIR>(v);
    }
}

__global__ __launch_bounds__(256) void k_pass2(float2* __restrict__ ZT, const bf16* __restrict__ wr,
                                               const bf16* __restrict__ wi, const float2* __restrict__ tw2048) {
    __shared__ float2 bufA[S_];
    __shared__ float2 bufB[S_];
    __shared__ float2 tw[S_];
    int k = blockIdx.x, j = threadIdx.x;
    float2* col = ZT + (size_t)k * S_;
    cf v[8];
#pragma unroll
    for (int r = 0; r < 8; ++r) v[r] = col[j + 256 * r];
#pragma unroll
    for (int r = 0; r < 8; ++r) tw[j + 256 * r] = tw2048[j + 256 * r];
    float wre[8], wim[8];
#pragma unroll
    for (int r = 0; r < 8; ++r) {
        wre[r] = bf2f(wr[(size_t)(j + 256 * r) * DF_ + k]);
        wim[r] = bf2f(wi[(size_t)(j + 256 * r) * DF_ + k]);
    }
    __syncthreads();
    fft2048_regs<-1>(v, bufA, bufB, tw, j);
#pragma unroll
    for (int r = 0; r < 8; ++r) v[r] = cmul(v[r], make_float2(wre[r], wim[r]));
    __syncthreads();
    fft2048_regs<1>(v, bufA, bufB, tw, j);
#pragma unroll
    for (int r = 0; r < 8; ++r) col[j + 256 * r] = v[r];
}

__global__ __launch_bounds__(320) void k_pass3(const float2* __restrict__ Z2T, const bf16* __restrict__ x,
                                               const float* __restrict__ mod, bf16* __restrict__ x1,
                                               bf16* __restrict__ h2) {
    __shared__ float2 buf0[4][D_];
    __shared__ float2 buf1[4][D_];
    __shared__ float2 tw[D_];
    int tid = threadIdx.x;
    int s0 = blockIdx.x * 8;
    for (int m = tid; m < D_; m += 320) {
        float sn, cs; sincospif(-2.f * (float)m / (float)D_, &sn, &cs);
        tw[m] = make_float2(cs, sn);
    }
    for (int task = tid; task < DF_ * 4; task += 320) {
        int p = task & 3, k = task >> 2;
        float4 q = *reinterpret_cast<const float4*>(Z2T + (size_t)k * S_ + s0 + 2 * p);
        if (k == 0 || k == 288) {
            buf0[p][k] = make_float2(q.x, q.z);
        } else {
            buf0[p][k] = make_float2(q.x - q.w, q.y + q.z);
            buf0[p][D_ - k] = make_float2(q.x + q.w, q.z - q.y);
        }
    }
    __syncthreads();
    int w, t;
    if (tid < 256) { w = tid >> 6; t = tid & 63; } else { w = (tid - 256) >> 3; t = 64 + ((tid - 256) & 7); }
    bool act = tid < 288;
    if (act) {
        cf v[8];
#pragma unroll
        for (int r = 0; r < 8; ++r) v[r] = buf0[w][t + 72 * r];
        dft8<1>(v);
#pragma unroll
        for (int r = 0; r < 8; ++r) buf1[w][8 * t + r] = v[r];
    }
    __syncthreads();
    if (act) {
        cf v[8];
        int k = t & 7;
#pragma unroll
        for (int r = 0; r < 8; ++r) v[r] = buf1[w][t + 72 * r];
#pragma unroll
        for (int r = 1; r < 8; ++r) v[r] = cmulc(v[r], tw[r * k * 9]);
        dft8<1>(v);
        int o = (t >> 3) * 64 + k;
#pragma unroll
        for (int r = 0; r < 8; ++r) buf0[w][o + 8 * r] = v[r];
    }
    __syncthreads();
    if (tid < 256) {
        int j = tid & 63;
        cf v[9];
#pragma unroll
        for (int r = 0; r < 9; ++r) v[r] = buf0[w][j + 64 * r];
#pragma unroll
        for (int r = 1; r < 9; ++r) v[r] = cmulc(v[r], tw[(r * j) % D_]);
        dft9<1>(v);
        const float scl = 1.f / ((float)S_ * (float)D_);
        int ra = s0 + 2 * w, rb = ra + 1;
        float xa[9], xb[9];
#pragma unroll
        for (int r = 0; r < 9; ++r) {
            int n = j + 64 * r;
            float g = mod[2 * D_ + n];
            float ya = rbf(v[r].x * scl), yb = rbf(v[r].y * scl);
            xa[r] = rbf(bf2f(x[(size_t)ra * D_ + n]) + rbf(g * ya));
            xb[r] = rbf(bf2f(x[(size_t)rb * D_ + n]) + rbf(g * yb));
        }
        float mba, dena, mbb, denb;
        ln_stats(xa, mba, dena);
        ln_stats(xb, mbb, denb);
#pragma unroll
        for (int r = 0; r < 9; ++r) {
            int n = j + 64 * r;
            float sh = mod[3 * D_ + n], sc1 = rbf(1.f + mod[4 * D_ + n]);
            x1[(size_t)ra * D_ + n] = f2bf(xa[r]);
            x1[(size_t)rb * D_ + n] = f2bf(xb[r]);
            h2[(size_t)ra * D_ + n] = f2bf(ln_apply(xa[r], mba, dena, sc1, sh));
            h2[(size_t)rb * D_ + n] = f2bf(ln_apply(xb[r], mbb, denb, sc1, sh));
        }
    }
}

__global__ void k_gemm1(const bf16* A, const bf16* B, bf16* hid) {
    int n = blockIdx.x * blockDim.x + threadIdx.x;
    int m = blockIdx.y;
    if (n >= F_) return;
    float acc = 0.f;
    for (int k = 0; k < D_; ++k) acc += bf2f(A[(size_t)m * D_ + k]) * bf2f(B[(size_t)k * F_ + n]);
    float hv = rbf(acc);
    hid[(size_t)m * F_ + n] = f2bf(hv / (1.f + __expf(-hv)));
}
__global__ void k_gemm2(const bf16* hid, const bf16* B, const bf16* x1, const float* gb, bf16* out) {
    int n = blockIdx.x * blockDim.x + threadIdx.x;
    int m = blockIdx.y;
    if (n >= D_) return;
    float acc = 0.f;
    for (int k = 0; k < F_; ++k) acc += bf2f(hid[(size_t)m * F_ + k]) * bf2f(B[(size_t)k * D_ + n]);
    float o = rbf(acc);
    float t = rbf(gb[n] * o);
    out[(size_t)m * D_ + n] = f2bf(bf2f(x1[(size_t)m * D_ + n]) + t);
}

extern "C" void kernel_launch(void* const* d_in, const int* in_sizes, int n_in,
                              void* d_out, int out_size, void* d_ws, size_t ws_size,
                              hipStream_t stream) {
    const bf16* x = (const bf16*)d_in[0];
    const bf16* wr = (const bf16*)d_in[1];
    const bf16* wi = (const bf16*)d_in[2];
    const bf16* w1 = (const bf16*)d_in[4];
    const bf16* w2 = (const bf16*)d_in[5];
    const bf16* c = (const bf16*)d_in[6];
    const bf16* wmod = (const bf16*)d_in[7];
    bf16* out = (bf16*)d_out;
    char* ws = (char*)d_ws;
    float* mod = (float*)ws; ws += 16384;
    float2* tw2048 = (float2*)ws; ws += 16384;
    float2* ZT = (float2*)ws; ws += (size_t)DF_ * S_ * 8 + 4096;
    bf16* x1 = (bf16*)ws; ws += (size_t)S_ * D_ * 2;
    bf16* h2 = (bf16*)ws; ws += (size_t)S_ * D_ * 2;
    bf16* hid = (bf16*)ws; ws += (size_t)S_ * F_ * 2;

    k_mod<<<116, 256, 0, stream>>>(c, wmod, mod, tw2048);
    k_pass1<<<S_ / 8, 320, 0, stream>>>(x, mod, ZT);
    k_pass2<<<DF_, 256, 0, stream>>>(ZT, wr, wi, tw2048);
    k_pass3<<<S_ / 8, 320, 0, stream>>>(ZT, x, mod, x1, h2);
    k_gemm1<<<dim3((F_ + 255) / 256, S_), 256, 0, stream>>>(h2, w1, hid);
    k_gemm2<<<dim3((D_ + 255) / 256, S_), 256, 0, stream>>>(hid, w2, x1, mod + 5 * D_, out);
}
```

```cpp
#include <hip/hip_runtime.h>
#include <hip/hip_bf16.h>

typedef __hip_bfloat16 bf16;
#define S_ 2048
#define D_ 576
#define DF_ 289
#define F_ 4608

__device__ __forceinline__ float bf2f(bf16 v) { return __bfloat162float(v); }
__device__ __forceinline__ bf16 f2bf(float v) { return __float2bfloat16(v); }
__device__ __forceinline__ float rbf(float v) { return bf2f(f2bf(v)); }
__device__ __forceinline__ float us2f(unsigned short u) { return __uint_as_float(((unsigned)u) << 16); }

typedef float2 cf;
__device__ __forceinline__ cf cadd(cf a, cf b) { return make_float2(a.x + b.x, a.y + b.y); }
__device__ __forceinline__ cf csub(cf a, cf b) { return make_float2(a.x - b.x, a.y - b.y); }
__device__ __forceinline__ cf cmul(cf a, cf b) { return make_float2(a.x * b.x - a.y * b.y, a.x * b.y + a.y * b.x); }
__device__ __forceinline__ cf cmulc(cf a, cf b) {
    return make_float2(a.x * b.x + a.y * b.y, a.y * b.x - a.x * b.y);
}
template <int DIR> __device__ __forceinline__ cf mulj(cf a) {
    return (DIR < 0) ? make_float2(a.y, -a.x) : make_float2(-a.y, a.x);
}
template <int DIR> __device__ __forceinline__ cf twmul(cf a, cf w) {
    return (DIR < 0) ? cmul(a, w) : cmulc(a, w);
}

template <int DIR> __device__ __forceinline__ void dft4(cf& v0, cf& v1, cf& v2, cf& v3) {
    cf t0 = cadd(v0, v2), t1 = csub(v0, v2), t2 = cadd(v1, v3), t3 = mulj<DIR>(csub(v1, v3));
    v0 = cadd(t0, t2); v2 = csub(t0, t2); v1 = cadd(t1, t3); v3 = csub(t1, t3);
}
template <int DIR> __device__ __forceinline__ void dft8(cf* v) {
    dft4<DIR>(v[0], v[2], v[4], v[6]);
    dft4<DIR>(v[1], v[3], v[5], v[7]);
    const float h = 0.70710678118654752440f;
    cf o1 = v[3], o3 = v[7];
    cf a1 = (DIR < 0) ? make_float2(h * (o1.x + o1.y), h * (o1.y - o1.x)) : make_float2(h * (o1.x - o1.y), h * (o1.y + o1.x));
    cf a2 = mulj<DIR>(v[5]);
    cf a3 = (DIR < 0) ? make_float2(h * (o3.y - o3.x), h * (-o3.x - o3.y)) : make_float2(h * (-o3.x - o3.y), h * (o3.x - o3.y));
    cf e0 = v[0], e1 = v[2], e2 = v[4], e3 = v[6], o0 = v[1];
    v[0] = cadd(e0, o0); v[4] = csub(e0, o0);
    v[1] = cadd(e1, a1); v[5] = csub(e1, a1);
    v[2] = cadd(e2, a2); v[6] = csub(e2, a2);
    v[3] = cadd(e3, a3); v[7] = csub(e3, a3);
}
template <int DIR> __device__ __forceinline__ void dft3(cf& v0, cf& v1, cf& v2) {
    const float c = -0.5f, s = 0.86602540378443864676f * DIR;
    cf t1 = cadd(v1, v2), t2 = csub(v1, v2);
    cf m = make_float2(v0.x + c * t1.x, v0.y + c * t1.y);
    cf r = make_float2(-s * t2.y, s * t2.x);
    v0 = cadd(v0, t1);
    v1 = cadd(m, r);
    v2 = csub(m, r);
}
template <int DIR> __device__ __forceinline__ void dft9(cf* v) {
    dft3<DIR>(v[0], v[3], v[6]);
    dft3<DIR>(v[1], v[4], v[7]);
    dft3<DIR>(v[2], v[5], v[8]);
    const float c1 = 0.76604444311897803520f, s1 = 0.64278760968653932632f * DIR;
    const float c2 = 0.17364817766693034885f, s2 = 0.98480775301220805937f * DIR;
    const float c4 = -0.93969262078590838405f, s4 = 0.34202014332566873304f * DIR;
    v[4] = cmul(v[4], make_float2(c1, s1));
    v[7] = cmul(v[7], make_float2(c2, s2));
    v[5] = cmul(v[5], make_float2(c2, s2));
    v[8] = cmul(v[8], make_float2(c4, s4));
    dft3<DIR>(v[0], v[1], v[2]);
    dft3<DIR>(v[3], v[4], v[5]);
    dft3<DIR>(v[6], v[7], v[8]);
    cf t;
    t = v[1]; v[1] = v[3]; v[3] = t;
    t = v[2]; v[2] = v[6]; v[6] = t;
    t = v[5]; v[5] = v[7]; v[7] = t;
}

__device__ __forceinline__ float wave_sum(float v) {
#pragma unroll
    for (int o = 32; o > 0; o >>= 1) v += __shfl_xor(v, o, 64);
    return v;
}

__global__ __launch_bounds__(256) void k_mod(const bf16* __restrict__ c, const bf16* __restrict__ wmod,
                                             float* __restrict__ mod, float2* __restrict__ tw2048) {
    int b = blockIdx.x, t = threadIdx.x;
    if (b >= 108) {
        int m = (b - 108) * 256 + t;
        float sn, cs; sincospif(-2.f * (float)m / 2048.f, &sn, &cs);
        tw2048[m] = make_float2(cs, sn);
        return;
    }
    __shared__ float red[64][33];
    int cg = t & 3, rg = t >> 2;
    float acc[8];
#pragma unroll
    for (int i = 0; i < 8; ++i) acc[i] = 0.f;
#pragma unroll
    for (int i = 0; i < 9; ++i) {
        int r = rg + 64 * i;
        float cv = bf2f(c[r]);
        float sv = rbf(cv / (1.f + __expf(-cv)));
        uint4 q = *reinterpret_cast<const uint4*>(wmod + (size_t)r * (6 * D_) + b * 32 + cg * 8);
        unsigned u[4] = {q.x, q.y, q.z, q.w};
#pragma unroll
        for (int j = 0; j < 4; ++j) {
            acc[2 * j] += sv * __uint_as_float(u[j] << 16);
            acc[2 * j + 1] += sv * __uint_as_float(u[j] & 0xffff0000u);
        }
    }
#pragma unroll
    for (int i = 0; i < 8; ++i) red[rg][cg * 8 + i] = acc[i];
    __syncthreads();
    if (t < 32) {
        float s = 0.f;
        for (int r = 0; r < 64; ++r) s += red[r][t];
        mod[b * 32 + t] = rbf(s);
    }
}

__device__ __forceinline__ void ln_stats(const float* v, float& mb, float& den) {
    float s = 0.f;
#pragma unroll
    for (int r = 0; r < 9; ++r) s += v[r];
    float mean = wave_sum(s) * (1.f / D_);
    float q = 0.f;
#pragma unroll
    for (int r = 0; r < 9; ++r) { float e = v[r] - mean; q += e * e; }
    float var = wave_sum(q) * (1.f / D_);
    mb = rbf(mean);
    den = rbf(sqrtf(rbf(rbf(var) + 1e-5f)));
}
__device__ __forceinline__ float ln_apply(float x, float mb, float den, float sc1, float sh) {
    float xm = rbf(x - mb);
    float n = rbf(xm / den);
    return rbf(rbf(n * sc1) + sh);
}

__global__ __launch_bounds__(320) void k_pass1(const bf16* __restrict__ x, const float* __restrict__ mod,
                                               float2* __restrict__ Z1T) {
    __shared__ float2 buf0[4][D_];
    __shared__ float2 buf1[4][D_];
    __shared__ float2 tw[D_];
    int tid = threadIdx.x;
    int s0 = blockIdx.x * 8;
    for (int m = tid; m < D_; m += 320) {
        float sn, cs; sincospif(-2.f * (float)m / (float)D_, &sn, &cs);
        tw[m] = make_float2(cs, sn);
    }
    if (tid < 256) {
        int w = tid >> 6, j = tid & 63;
        const bf16* xa = x + (size_t)(s0 + 2 * w) * D_;
        const bf16* xb = xa + D_;
        float va[9], vb[9];
#pragma unroll
        for (int r = 0; r < 9; ++r) { va[r] = bf2f(xa[j + 64 * r]); vb[r] = bf2f(xb[j + 64 * r]); }
        float mba, dena, mbb, denb;
        ln_stats(va, mba, dena);
        ln_stats(vb, mbb, denb);
        cf v[9];
#pragma unroll
        for (int r = 0; r < 9; ++r) {
            int n = j + 64 * r;
            float sh = mod[n], sc1 = rbf(1.f + mod[D_ + n]);
            v[r] = make_float2(ln_apply(va[r], mba, dena, sc1, sh), ln_apply(vb[r], mbb, denb, sc1, sh));
        }
        dft9<-1>(v);
#pragma unroll
        for (int r = 0; r < 9; ++r) buf0[w][9 * j + r] = v[r];
    }
    __syncthreads();
    int w, t;
    if (tid < 256) { w = tid >> 6; t = tid & 63; } else { w = (tid - 256) >> 3; t = 64 + ((tid - 256) & 7); }
    bool act = tid < 288;
    if (act) {
        cf v[8];
        int k = t % 9;
#pragma unroll
        for (int r = 0; r < 8; ++r) v[r] = buf0[w][t + 72 * r];
#pragma unroll
        for (int r = 1; r < 8; ++r) v[r] = cmul(v[r], tw[r * k * 8]);
        dft8<-1>(v);
        int o = (t / 9) * 72 + k;
#pragma unroll
        for (int r = 0; r < 8; ++r) buf1[w][o + 9 * r] = v[r];
    }
    __syncthreads();
    if (act) {
        cf v[8];
#pragma unroll
        for (int r = 0; r < 8; ++r) v[r] = buf1[w][t + 72 * r];
#pragma unroll
        for (int r = 1; r < 8; ++r) v[r] = cmul(v[r], tw[r * t]);
        dft8<-1>(v);
#pragma unroll
        for (int r = 0; r < 8; ++r) buf0[w][t + 72 * r] = v[r];
    }
    __syncthreads();
    for (int task = tid; task < DF_ * 4; task += 320) {
        int p = task & 3, k = task >> 2;
        cf zk = buf0[p][k];
        cf zm = buf0[p][(D_ - k) % D_];
        float4 o;
        o.x = 0.5f * (zk.x + zm.x);
        o.y = 0.5f * (zk.y - zm.y);
        o.z = 0.5f * (zk.y + zm.y);
        o.w = -0.5f * (zk.x - zm.x);
        *reinterpret_cast<float4*>(Z1T + (size_t)k * S_ + s0 + 2 * p) = o;
    }
}

template <int DIR>
__device__ __forceinline__ void fft2048_regs(cf* v, float2* bufA, float2* bufB, const float2* tw, int j) {
    dft4<DIR>(v[0], v[2], v[4], v[6]);
    dft4<DIR>(v[1], v[3], v[5], v[7]);
#pragma unroll
    for (int r = 0; r < 4; ++r) { bufA[4 * j + r] = v[2 * r]; bufA[4 * (j + 256) + r] = v[2 * r + 1]; }
    __syncthreads();
    {
        int k = j & 3;
#pragma unroll
        for (int r = 0; r < 8; ++r) v[r] = bufA[j + 256 * r];
#pragma unroll
        for (int r = 1; r < 8; ++r) v[r] = twmul<DIR>(v[r], tw[r * k * 64]);
        dft8<DIR>(v);
        int o = (j >> 2) * 32 + k;
#pragma unroll
        for (int r = 0; r < 8; ++r) bufB[o + 4 * r] = v[r];
    }
    __syncthreads();
    {
        int k = j & 31;
#pragma unroll
        for (int r = 0; r < 8; ++r) v[r] = bufB[j + 256 * r];
#pragma unroll
        for (int r = 1; r < 8; ++r) v[r] = twmul<DIR>(v[r], tw[r * k * 8]);
        dft8<DIR>(v);
        int o = (j >> 5) * 256 + k;
#pragma unroll
        for (int r = 0; r < 8; ++r) bufA[o + 32 * r] = v[r];
    }
    __syncthreads();
    {
#pragma unroll
        for (int r = 0; r < 8; ++r) v[r] = bufA[j + 256 * r];
#pragma unroll
        for (int r = 1; r < 8; ++r) v[r] = twmul<DIR>(v[r], tw[r * j]);
        dft8<DIR>(v);
    }
}

__global__ __launch_bounds__(256) void k_pass2(float2* __restrict__ ZT, const bf16* __restrict__ wr,
                                               const bf16* __restrict__ wi, const float2* __restrict__ tw2048) {
    __shared__ float2 bufA[S_];
    __shared__ float2 bufB[S_];
    __shared__ float2 tw[S_];
    int k = blockIdx.x, j = threadIdx.x;
    float2* col = ZT + (size_t)k * S_;
    cf v[8];
#pragma unroll
    for (int r = 0; r < 8; ++r) v[r] = col[j + 256 * r];
#pragma unroll
    for (int r = 0; r < 8; ++r) tw[j + 256 * r] = tw2048[j + 256 * r];
    float wre[8], wim[8];
#pragma unroll
    for (int r = 0; r < 8; ++r) {
        wre[r] = bf2f(wr[(size_t)(j + 256 * r) * DF_ + k]);
        wim[r] = bf2f(wi[(size_t)(j + 256 * r) * DF_ + k]);
    }
    __syncthreads();
    fft2048_regs<-1>(v, bufA, bufB, tw, j);
#pragma unroll
    for (int r = 0; r < 8; ++r) v[r] = cmul(v[r], make_float2(wre[r], wim[r]));
    __syncthreads();
    fft2048_regs<1>(v, bufA, bufB, tw, j);
#pragma unroll
    for (int r = 0; r < 8; ++r) col[j + 256 * r] = v[r];
}

__global__ __launch_bounds__(320) void k_pass3(const float2* __restrict__ Z2T, const bf16* __restrict__ x,
                                               const float* __restrict__ mod, bf16* __restrict__ x1,
                                               bf16* __restrict__ h2) {
    __shared__ float2 buf0[4][D_];
    __shared__ float2 buf1[4][D_];
    __shared__ float2 tw[D_];
    int tid = threadIdx.x;
    int s0 = blockIdx.x * 8;
    for (int m = tid; m < D_; m += 320) {
        float sn, cs; sincospif(-2.f * (float)m / (float)D_, &sn, &cs);
        tw[m] = make_float2(cs, sn);
    }
    for (int task = tid; task < DF_ * 4; task += 320) {
        int p = task & 3, k = task >> 2;
        float4 q = *reinterpret_cast<const float4*>(Z2T + (size_t)k * S_ + s0 + 2 * p);
        if (k == 0 || k == 288) {
            buf0[p][k] = make_float2(q.x, q.z);
        } else {
            buf0[p][k] = make_float2(q.x - q.w, q.y + q.z);
            buf0[p][D_ - k] = make_float2(q.x + q.w, q.z - q.y);
        }
    }
    __syncthreads();
    int w, t;
    if (tid < 256) { w = tid >> 6; t = tid & 63; } else { w = (tid - 256) >> 3; t = 64 + ((tid - 256) & 7); }
    bool act = tid < 288;
    if (act) {
        cf v[8];
#pragma unroll
        for (int r = 0; r < 8; ++r) v[r] = buf0[w][t + 72 * r];
        dft8<1>(v);
#pragma unroll
        for (int r = 0; r < 8; ++r) buf1[w][8 * t + r] = v[r];
    }
    __syncthreads();
    if (act) {
        cf v[8];
        int k = t & 7;
#pragma unroll
        for (int r = 0; r < 8; ++r) v[r] = buf1[w][t + 72 * r];
#pragma unroll
        for (int r = 1; r < 8; ++r) v[r] = cmulc(v[r], tw[r * k * 9]);
        dft8<1>(v);
        int o = (t >> 3) * 64 + k;
#pragma unroll
        for (int r = 0; r < 8; ++r) buf0[w][o + 8 * r] = v[r];
    }
    __syncthreads();
    if (tid < 256) {
        int j = tid & 63;
        cf v[9];
#pragma unroll
        for (int r = 0; r < 9; ++r) v[r] = buf0[w][j + 64 * r];
#pragma unroll
        for (int r = 1; r < 9; ++r) v[r] = cmulc(v[r], tw[(r * j) % D_]);
        dft9<1>(v);
        const float scl = 1.f / ((float)S_ * (float)D_);
        int ra = s0 + 2 * w, rb = ra + 1;
        float xa[9], xb[9];
#pragma unroll
        for (int r = 0; r < 9; ++r) {
            int n = j + 64 * r;
            float g = mod[2 * D_ + n];
            float ya = rbf(v[r].x * scl), yb = rbf(v[r].y * scl);
            xa[r] = rbf(bf2f(x[(size_t)ra * D_ + n]) + rbf(g * ya));
            xb[r] = rbf(bf2f(x[(size_t)rb * D_ + n]) + rbf(g * yb));
        }
        float mba, dena, mbb, denb;
        ln_stats(xa, mba, dena);
        ln_stats(xb, mbb, denb);
#pragma unroll
        for (int r = 0; r < 9; ++r) {
            int n = j + 64 * r;
            float sh = mod[3 * D_ + n], sc1 = rbf(1.f + mod[4 * D_ + n]);
            x1[(size_t)ra * D_ + n] = f2bf(xa[r]);
            x1[(size_t)rb * D_ + n] = f2bf(xb[r]);
            h2[(size_t)ra * D_ + n] = f2bf(ln_apply(xa[r], mba, dena, sc1, sh));
            h2[(size_t)rb * D_ + n] = f2bf(ln_apply(xb[r], mbb, denb, sc1, sh));
        }
    }
}

typedef __attribute__((ext_vector_type(8))) short bf16x8;
typedef __attribute__((ext_vector_type(4))) short s16x4;
typedef __attribute__((ext_vector_type(4))) float f32x4;
typedef __attribute__((address_space(3))) s16x4* lds_s16x4_ptr;
constexpr int G_ASZ = 256 * 64 * 2, G_BSZ = 64 * 144 * 2, G_STAGE = G_ASZ + G_BSZ, G_LDS = G_STAGE * 3;

__device__ __forceinline__ s16x4 lds_tr_read(const char* p) {
    return __builtin_amdgcn_ds_read_tr16_b64_v4i16((lds_s16x4_ptr)(p));
}
__device__ __forceinline__ void glds16(const void* gsrc, unsigned lds_dst) {
    unsigned keep;
    asm volatile("s_mov_b32 %0, m0\n\ts_mov_b32 m0, %2\n\ts_nop 0\n\tglobal_load_lds_dwordx4 %1, off\n\ts_mov_b32 m0, %0"
                 : "=&s"(keep) : "v"(gsrc), "s"(lds_dst) : "memory");
}
template <int LDA, int LDB>
__device__ __forceinline__ void g_issue(const bf16* __restrict__ A, const bf16* __restrict__ B, char* stage,
                                        int kt, int lw, int lane) {
#pragma unroll
    for (int i = 0; i < 8; ++i) {
        int ins = lw * 8 + i;
        int row = ins * 8 + (lane >> 3);
        int c = (lane & 7) ^ ((row >> 1) & 7);
        const bf16* src = A + (size_t)row * LDA + kt * 64 + c * 8;
        glds16(src, __builtin_amdgcn_readfirstlane((unsigned)(size_t)stage + ins * 1024));
    }
    int nb = (lw < 2) ? 5 : 4;
    int b0 = (lw < 2) ? lw * 5 : 10 + (lw - 2) * 4;
#pragma unroll
    for (int i = 0; i < 5; ++i) {
        if (i < nb) {
            int ins = b0 + i;
            int off = ins * 1024 + lane * 16;
            int row = off / 288, cb = off % 288;
            const bf16* src = B + (size_t)(kt * 64 + row) * LDB + (cb >> 1);
            glds16(src, __builtin_amdgcn_readfirstlane((unsigned)(size_t)stage + G_ASZ + ins * 1024));
        }
    }
}

template <int MODE, int LDA, int LDB>
__global__ __launch_bounds__(768, 3) void k_gemm(const bf16* __restrict__ Ag, const bf16* __restrict__ Bg,
                                                 bf16* __restrict__ C, float* __restrict__ P) {
    extern __shared__ __attribute__((aligned(16))) char smem[];
    constexpr int NT = 9;
    int tid = threadIdx.x;
    int lane = tid & 63;
    int w = __builtin_amdgcn_readfirstlane(tid >> 6);
    int bid = blockIdx.x;
    int xcd = bid & 7, idx = bid >> 3;
    int brow, bcol, k0;
    if (MODE == 0) { int bn = xcd * 4 + (idx & 3), bm = idx >> 2; brow = bm * 256; bcol = bn * 144; k0 = 0; }
    else { int bn = idx & 3, bm = idx >> 2; brow = bm * 256; bcol = bn * 144; k0 = xcd * 576; }
    const bf16* A = Ag + (size_t)brow * LDA + k0;
    const bf16* B = Bg + (size_t)k0 * LDB + bcol;

    if (w >= 8) {
        int lw = w - 8;
        g_issue<LDA, LDB>(A, B, smem + 0 * G_STAGE, 0, lw, lane);
        g_issue<LDA, LDB>(A, B, smem + 1 * G_STAGE, 1, lw, lane);
        for (int t = 0; t < NT; ++t) {
            if (t + 1 < NT) {
                if (lw < 2) asm volatile("s_waitcnt vmcnt(13)" ::: "memory");
                else asm volatile("s_waitcnt vmcnt(12)" ::: "memory");
            } else {
                asm volatile("s_waitcnt vmcnt(0)" ::: "memory");
            }
            __builtin_amdgcn_s_barrier();
            if (t + 2 < NT) g_issue<LDA, LDB>(A, B, smem + ((t + 2) % 3) * G_STAGE, t + 2, lw, lane);
        }
        return;
    }
    f32x4 acc[2][9];
#pragma unroll
    for (int i = 0; i < 2; ++i)
#pragma unroll
        for (int j = 0; j < 9; ++j) acc[i][j] = (f32x4){0.f, 0.f, 0.f, 0.f};
    int g = lane >> 4, fr = lane & 15, q = fr >> 2, p = lane & 3;
    int a_off[2][2][2];
#pragma unroll
    for (int sl = 0; sl < 2; ++sl)
#pragma unroll
        for (int mi = 0; mi < 2; ++mi) {
            int row = w * 32 + mi * 16 + fr;
            int c = sl * 4 + (g >> 1);
            int sw = (row >> 1) & 7;
            a_off[sl][mi][0] = row * 128 + ((c ^ sw) << 4) + (g & 1) * 8;
            a_off[sl][mi][1] = row * 128 + (((c + 2) ^ sw) << 4) + (g & 1) * 8;
        }
    int b_off = G_ASZ + (4 * g + q) * 288 + p * 8;
    for (int t = 0; t < NT; ++t) {
        __builtin_amdgcn_s_barrier();
        const char* st = smem + (t % 3) * G_STAGE;
#pragma unroll
        for (int sl = 0; sl < 2; ++sl) {
            bf16x8 af[2], bfr[9];
#pragma unroll
            for (int mi = 0; mi < 2; ++mi) {
                s16x4 lo = *reinterpret_cast<const s16x4*>(st + a_off[sl][mi][0]);
                s16x4 hi = *reinterpret_cast<const s16x4*>(st + a_off[sl][mi][1]);
                af[mi] = __builtin_shufflevector(lo, hi, 0, 1, 2, 3, 4, 5, 6, 7);
            }
#pragma unroll
            for (int ni = 0; ni < 9; ++ni) {
                s16x4 lo = lds_tr_read(st + b_off + sl * 32 * 288 + ni * 32);
                s16x4 hi = lds_tr_read(st + b_off + sl * 32 * 288 + ni * 32 + 16 * 288);
                bfr[ni] = __builtin_shufflevector(lo, hi, 0, 1, 2, 3, 4, 5, 6, 7);
            }
            __builtin_amdgcn_sched_barrier(0);
#pragma unroll
            for (int ni = 0; ni < 9; ++ni)
#pragma unroll
                for (int mi = 0; mi < 2; ++mi)
                    acc[mi][ni] = __builtin_amdgcn_mfma_f32_16x16x32_bf16(bfr[ni], af[mi], acc[mi][ni], 0, 0, 0);
            __builtin_amdgcn_sched_barrier(0);
        }
    }
    if (MODE == 0) {
        char* stg = smem + w * 9216;
#pragma unroll
        for (int mi = 0; mi < 2; ++mi)
#pragma unroll
            for (int ni = 0; ni < 9; ++ni) {
                s16x4 pk;
#pragma unroll
                for (int r = 0; r < 4; ++r) {
                    float hv = rbf(acc[mi][ni][r]);
                    float sv = hv * __builtin_amdgcn_rcpf(1.f + __expf(-hv));
                    pk[r] = __builtin_bit_cast(short, f2bf(sv));
                }
                *reinterpret_cast<s16x4*>(stg + (mi * 16 + fr) * 288 + (ni * 16 + 4 * g) * 2) = pk;
            }
#pragma unroll
        for (int i = 0; i < 9; ++i) {
            int ch = lane + 64 * i;
            int row = ch / 18, cc = ch % 18;
            uint4 v = *reinterpret_cast<const uint4*>(stg + row * 288 + cc * 16);
            *reinterpret_cast<uint4*>(C + (size_t)(brow + w * 32 + row) * LDB + bcol + cc * 8) = v;
        }
    } else {
        float* Pp = P + (size_t)xcd * S_ * D_;
#pragma unroll
        for (int mi = 0; mi < 2; ++mi)
#pragma unroll
            for (int ni = 0; ni < 9; ++ni)
                *reinterpret_cast<f32x4*>(Pp + (size_t)(brow + w * 32 + mi * 16 + fr) * LDB + bcol + ni * 16 + 4 * g) = acc[mi][ni];
    }
}

__global__ __launch_bounds__(256) void k_final(const float* __restrict__ P, const bf16* __restrict__ x1,
                                               const float* __restrict__ gb, bf16* __restrict__ out) {
    int i = (blockIdx.x * 256 + threadIdx.x) * 4;
    float4 a = *reinterpret_cast<const float4*>(P + i);
#pragma unroll
    for (int ks = 1; ks < 8; ++ks) {
        float4 b = *reinterpret_cast<const float4*>(P + (size_t)ks * S_ * D_ + i);
        a.x += b.x; a.y += b.y; a.z += b.z; a.w += b.w;
    }
    int n = i % D_;
    float v[4] = {a.x, a.y, a.z, a.w};
    s16x4 xv = *reinterpret_cast<const s16x4*>(x1 + i);
    s16x4 o;
#pragma unroll
    for (int r = 0; r < 4; ++r) {
        float t = rbf(gb[n + r] * rbf(v[r]));
        float xf = __uint_as_float(((unsigned)(unsigned short)xv[r]) << 16);
        o[r] = __builtin_bit_cast(short, f2bf(xf + t));
    }
    *reinterpret_cast<s16x4*>(out + i) = o;
}

extern "C" void kernel_launch(void* const* d_in, const int* in_sizes, int n_in,
                              void* d_out, int out_size, void* d_ws, size_t ws_size,
                              hipStream_t stream) {
    const bf16* x = (const bf16*)d_in[0];
    const bf16* wr = (const bf16*)d_in[1];
    const bf16* wi = (const bf16*)d_in[2];
    const bf16* w1 = (const bf16*)d_in[4];
    const bf16* w2 = (const bf16*)d_in[5];
    const bf16* c = (const bf16*)d_in[6];
    const bf16* wmod = (const bf16*)d_in[7];
    bf16* out = (bf16*)d_out;
    char* ws = (char*)d_ws;
    float* mod = (float*)ws; ws += 16384;
    float2* tw2048 = (float2*)ws; ws += 16384;
    float2* ZT = (float2*)ws; ws += (size_t)DF_ * S_ * 8 + 4096;
    bf16* x1 = (bf16*)ws; ws += (size_t)S_ * D_ * 2;
    bf16* h2 = (bf16*)ws; ws += (size_t)S_ * D_ * 2;
    bf16* hid = (bf16*)ws; ws += (size_t)S_ * F_ * 2;
    float* P = (float*)ws; ws += (size_t)8 * S_ * D_ * 4;

    hipFuncSetAttribute((const void*)k_gemm<0, D_, F_>, hipFuncAttributeMaxDynamicSharedMemorySize, G_LDS);
    hipFuncSetAttribute((const void*)k_gemm<1, F_, D_>, hipFuncAttributeMaxDynamicSharedMemorySize, G_LDS);
    k_mod<<<116, 256, 0, stream>>>(c, wmod, mod, tw2048);
    k_pass1<<<S_ / 8, 320, 0, stream>>>(x, mod, ZT);
    k_pass2<<<DF_, 256, 0, stream>>>(ZT, wr, wi, tw2048);
    k_pass3<<<S_ / 8, 320, 0, stream>>>(ZT, x, mod, x1, h2);
    k_gemm<0, D_, F_><<<256, 768, G_LDS, stream>>>(h2, w1, hid, nullptr);
    k_gemm<1, F_, D_><<<256, 768, G_LDS, stream>>>(hid, w2, nullptr, P);
    k_final<<<S_ * D_ / 1024, 256, 0, stream>>>(P, x1, mod + 5 * D_, out);
}
```

```cpp
#include <hip/hip_runtime.h>
#include <hip/hip_bf16.h>

typedef __hip_bfloat16 bf16;
#define S_ 2048
#define D_ 576
#define DF_ 289
#define F_ 4608

__device__ __forceinline__ float bf2f(bf16 v) { return __bfloat162float(v); }
__device__ __forceinline__ bf16 f2bf(float v) { return __float2bfloat16(v); }
__device__ __forceinline__ float rbf(float v) { return bf2f(f2bf(v)); }
__device__ __forceinline__ float us2f(unsigned short u) { return __uint_as_float(((unsigned)u) << 16); }

typedef float2 cf;
__device__ __forceinline__ cf cadd(cf a, cf b) { return make_float2(a.x + b.x, a.y + b.y); }
__device__ __forceinline__ cf csub(cf a, cf b) { return make_float2(a.x - b.x, a.y - b.y); }
__device__ __forceinline__ cf cmul(cf a, cf b) { return make_float2(a.x * b.x - a.y * b.y, a.x * b.y + a.y * b.x); }
__device__ __forceinline__ cf cmulc(cf a, cf b) {
    return make_float2(a.x * b.x + a.y * b.y, a.y * b.x - a.x * b.y);
}
template <int DIR> __device__ __forceinline__ cf mulj(cf a) {
    return (DIR < 0) ? make_float2(a.y, -a.x) : make_float2(-a.y, a.x);
}
template <int DIR> __device__ __forceinline__ cf twmul(cf a, cf w) {
    return (DIR < 0) ? cmul(a, w) : cmulc(a, w);
}

template <int DIR> __device__ __forceinline__ void dft4(cf& v0, cf& v1, cf& v2, cf& v3) {
    cf t0 = cadd(v0, v2), t1 = csub(v0, v2), t2 = cadd(v1, v3), t3 = mulj<DIR>(csub(v1, v3));
    v0 = cadd(t0, t2); v2 = csub(t0, t2); v1 = cadd(t1, t3); v3 = csub(t1, t3);
}
template <int DIR> __device__ __forceinline__ void dft8(cf* v) {
    dft4<DIR>(v[0], v[2], v[4], v[6]);
    dft4<DIR>(v[1], v[3], v[5], v[7]);
    const float h = 0.70710678118654752440f;
    cf o1 = v[3], o3 = v[7];
    cf a1 = (DIR < 0) ? make_float2(h * (o1.x + o1.y), h * (o1.y - o1.x)) : make_float2(h * (o1.x - o1.y), h * (o1.y + o1.x));
    cf a2 = mulj<DIR>(v[5]);
    cf a3 = (DIR < 0) ? make_float2(h * (o3.y - o3.x), h * (-o3.x - o3.y)) : make_float2(h * (-o3.x - o3.y), h * (o3.x - o3.y));
    cf e0 = v[0], e1 = v[2], e2 = v[4], e3 = v[6], o0 = v[1];
    v[0] = cadd(e0, o0); v[4] = csub(e0, o0);
    v[1] = cadd(e1, a1); v[5] = csub(e1, a1);
    v[2] = cadd(e2, a2); v[6] = csub(e2, a2);
    v[3] = cadd(e3, a3); v[7] = csub(e3, a3);
}
template <int DIR> __device__ __forceinline__ void dft3(cf& v0, cf& v1, cf& v2) {
    const float c = -0.5f, s = 0.86602540378443864676f * DIR;
    cf t1 = cadd(v1, v2), t2 = csub(v1, v2);
    cf m = make_float2(v0.x + c * t1.x, v0.y + c * t1.y);
    cf r = make_float2(-s * t2.y, s * t2.x);
    v0 = cadd(v0, t1);
    v1 = cadd(m, r);
    v2 = csub(m, r);
}
template <int DIR> __device__ __forceinline__ void dft9(cf* v) {
    dft3<DIR>(v[0], v[3], v[6]);
    dft3<DIR>(v[1], v[4], v[7]);
    dft3<DIR>(v[2], v[5], v[8]);
    const float c1 = 0.76604444311897803520f, s1 = 0.64278760968653932632f * DIR;
    const float c2 = 0.17364817766693034885f, s2 = 0.98480775301220805937f * DIR;
    const float c4 = -0.93969262078590838405f, s4 = 0.34202014332566873304f * DIR;
    v[4] = cmul(v[4], make_float2(c1, s1));
    v[7] = cmul(v[7], make_float2(c2, s2));
    v[5] = cmul(v[5], make_float2(c2, s2));
    v[8] = cmul(v[8], make_float2(c4, s4));
    dft3<DIR>(v[0], v[1], v[2]);
    dft3<DIR>(v[3], v[4], v[5]);
    dft3<DIR>(v[6], v[7], v[8]);
    cf t;
    t = v[1]; v[1] = v[3]; v[3] = t;
    t = v[2]; v[2] = v[6]; v[6] = t;
    t = v[5]; v[5] = v[7]; v[7] = t;
}

__device__ __forceinline__ float wave_sum(float v) {
#pragma unroll
    for (int o = 32; o > 0; o >>= 1) v += __shfl_xor(v, o, 64);
    return v;
}

__global__ __launch_bounds__(256) void k_mod(const bf16* __restrict__ c, const bf16* __restrict__ wmod,
                                             float* __restrict__ mod, float2* __restrict__ tw2048,
                                             const bf16* __restrict__ wr, const bf16* __restrict__ wi,
                                             unsigned* __restrict__ WT) {
    int b = blockIdx.x, t = threadIdx.x;
    if (b >= 116) {
        __shared__ unsigned tile[32 * 289];
        int s0 = (b - 116) * 32;
        const unsigned short* pr = reinterpret_cast<const unsigned short*>(wr) + (size_t)s0 * DF_;
        const unsigned short* pi = reinterpret_cast<const unsigned short*>(wi) + (size_t)s0 * DF_;
        for (int i = t; i < 32 * DF_; i += 256) tile[i] = (unsigned)pr[i] | ((unsigned)pi[i] << 16);
        __syncthreads();
        for (int i = t; i < 32 * DF_; i += 256) {
            int k = i >> 5, sl = i & 31;
            WT[(size_t)k * S_ + s0 + sl] = tile[sl * DF_ + k];
        }
        return;
    }
    if (b >= 108) {
        int m = (b - 108) * 256 + t;
        float sn, cs; sincospif(-2.f * (float)m / 2048.f, &sn, &cs);
        tw2048[m] = make_float2(cs, sn);
        return;
    }
    __shared__ float red[64][33];
    int cg = t & 3, rg = t >> 2;
    float acc[8];
#pragma unroll
    for (int i = 0; i < 8; ++i) acc[i] = 0.f;
#pragma unroll
    for (int i = 0; i < 9; ++i) {
        int r = rg + 64 * i;
        float cv = bf2f(c[r]);
        float sv = rbf(cv / (1.f + __expf(-cv)));
        uint4 q = *reinterpret_cast<const uint4*>(wmod + (size_t)r * (6 * D_) + b * 32 + cg * 8);
        unsigned u[4] = {q.x, q.y, q.z, q.w};
#pragma unroll
        for (int j = 0; j < 4; ++j) {
            acc[2 * j] += sv * __uint_as_float(u[j] << 16);
            acc[2 * j + 1] += sv * __uint_as_float(u[j] & 0xffff0000u);
        }
    }
#pragma unroll
    for (int i = 0; i < 8; ++i) red[rg][cg * 8 + i] = acc[i];
    __syncthreads();
    if (t < 32) {
        float s = 0.f;
        for (int r = 0; r < 64; ++r) s += red[r][t];
        mod[b * 32 + t] = rbf(s);
    }
}

__device__ __forceinline__ void ln_stats(const float* v, float& mb, float& den) {
    float s = 0.f;
#pragma unroll
    for (int r = 0; r < 9; ++r) s += v[r];
    float mean = wave_sum(s) * (1.f / D_);
    float q = 0.f;
#pragma unroll
    for (int r = 0; r < 9; ++r) { float e = v[r] - mean; q += e * e; }
    float var = wave_sum(q) * (1.f / D_);
    mb = rbf(mean);
    den = rbf(sqrtf(rbf(rbf(var) + 1e-5f)));
}
__device__ __forceinline__ float ln_apply(float x, float mb, float den, float sc1, float sh) {
    float xm = rbf(x - mb);
    float n = rbf(xm / den);
    return rbf(rbf(n * sc1) + sh);
}

__global__ __launch_bounds__(320) void k_pass1(const bf16* __restrict__ x, const float* __restrict__ mod,
                                               float2* __restrict__ Z1T) {
    __shared__ float2 buf0[4][D_];
    __shared__ float2 buf1[4][D_];
    __shared__ float2 tw[D_];
    int tid = threadIdx.x;
    int s0 = blockIdx.x * 8;
    for (int m = tid; m < D_; m += 320) {
        float sn, cs; sincospif(-2.f * (float)m / (float)D_, &sn, &cs);
        tw[m] = make_float2(cs, sn);
    }
    if (tid < 256) {
        int w = tid >> 6, j = tid & 63;
        const bf16* xa = x + (size_t)(s0 + 2 * w) * D_;
        const bf16* xb = xa + D_;
        float va[9], vb[9];
#pragma unroll
        for (int r = 0; r < 9; ++r) { va[r] = bf2f(xa[j + 64 * r]); vb[r] = bf2f(xb[j + 64 * r]); }
        float mba, dena, mbb, denb;
        ln_stats(va, mba, dena);
        ln_stats(vb, mbb, denb);
        cf v[9];
#pragma unroll
        for (int r = 0; r < 9; ++r) {
            int n = j + 64 * r;
            float sh = mod[n], sc1 = rbf(1.f + mod[D_ + n]);
            v[r] = make_float2(ln_apply(va[r], mba, dena, sc1, sh), ln_apply(vb[r], mbb, denb, sc1, sh));
        }
        dft9<-1>(v);
#pragma unroll
        for (int r = 0; r < 9; ++r) buf0[w][9 * j + r] = v[r];
    }
    __syncthreads();
    int w, t;
    if (tid < 256) { w = tid >> 6; t = tid & 63; } else { w = (tid - 256) >> 3; t = 64 + ((tid - 256) & 7); }
    bool act = tid < 288;
    if (act) {
        cf v[8];
        int k = t % 9;
#pragma unroll
        for (int r = 0; r < 8; ++r) v[r] = buf0[w][t + 72 * r];
#pragma unroll
        for (int r = 1; r < 8; ++r) v[r] = cmul(v[r], tw[r * k * 8]);
        dft8<-1>(v);
        int o = (t / 9) * 72 + k;
#pragma unroll
        for (int r = 0; r < 8; ++r) buf1[w][o + 9 * r] = v[r];
    }
    __syncthreads();
    if (act) {
        cf v[8];
#pragma unroll
        for (int r = 0; r < 8; ++r) v[r] = buf1[w][t + 72 * r];
#pragma unroll
        for (int r = 1; r < 8; ++r) v[r] = cmul(v[r], tw[r * t]);
        dft8<-1>(v);
#pragma unroll
        for (int r = 0; r < 8; ++r) buf0[w][t + 72 * r] = v[r];
    }
    __syncthreads();
    for (int task = tid; task < DF_ * 4; task += 320) {
        int p = task & 3, k = task >> 2;
        cf zk = buf0[p][k];
        cf zm = buf0[p][(D_ - k) % D_];
        float4 o;
        o.x = 0.5f * (zk.x + zm.x);
        o.y = 0.5f * (zk.y - zm.y);
        o.z = 0.5f * (zk.y + zm.y);
        o.w = -0.5f * (zk.x - zm.x);
        *reinterpret_cast<float4*>(Z1T + (size_t)k * S_ + s0 + 2 * p) = o;
    }
}

template <int DIR>
__device__ __forceinline__ void fft2048_regs(cf* v, float2* bufA, float2* bufB, const float2* tw, int j) {
    dft4<DIR>(v[0], v[2], v[4], v[6]);
    dft4<DIR>(v[1], v[3], v[5], v[7]);
#pragma unroll
    for (int r = 0; r < 4; ++r) { bufA[4 * j + r] = v[2 * r]; bufA[4 * (j + 256) + r] = v[2 * r + 1]; }
    __syncthreads();
    {
        int k = j & 3;
#pragma unroll
        for (int r = 0; r < 8; ++r) v[r] = bufA[j + 256 * r];
#pragma unroll
        for (int r = 1; r < 8; ++r) v[r] = twmul<DIR>(v[r], tw[r * k * 64]);
        dft8<DIR>(v);
        int o = (j >> 2) * 32 + k;
#pragma unroll
        for (int r = 0; r < 8; ++r) bufB[o + 4 * r] = v[r];
    }
    __syncthreads();
    {
        int k = j & 31;
#pragma unroll
        for (int r = 0; r < 8; ++r) v[r] = bufB[j + 256 * r];
#pragma unroll
        for (int r = 1; r < 8; ++r) v[r] = twmul<DIR>(v[r], tw[r * k * 8]);
        dft8<DIR>(v);
        int o = (j >> 5) * 256 + k;
#pragma unroll
        for (int r = 0; r < 8; ++r) bufA[o + 32 * r] = v[r];
    }
    __syncthreads();
    {
#pragma unroll
        for (int r = 0; r < 8; ++r) v[r] = bufA[j + 256 * r];
#pragma unroll
        for (int r = 1; r < 8; ++r) v[r] = twmul<DIR>(v[r], tw[r * j]);
        dft8<DIR>(v);
    }
}

__global__ __launch_bounds__(256) void k_pass2(float2* __restrict__ ZT, const unsigned* __restrict__ WT,
                                               const float2* __restrict__ tw2048) {
    __shared__ float2 bufA[S_];
    __shared__ float2 bufB[S_];
    __shared__ float2 tw[S_];
    int k = blockIdx.x, j = threadIdx.x;
    float2* col = ZT + (size_t)k * S_;
    cf v[8];
#pragma unroll
    for (int r = 0; r < 8; ++r) v[r] = col[j + 256 * r];
#pragma unroll
    for (int r = 0; r < 8; ++r) tw[j + 256 * r] = tw2048[j + 256 * r];
    float wre[8], wim[8];
#pragma unroll
    for (int r = 0; r < 8; ++r) {
        unsigned u = WT[(size_t)k * S_ + j + 256 * r];
        wre[r] = __uint_as_float(u << 16);
        wim[r] = __uint_as_float(u & 0xffff0000u);
    }
    __syncthreads();
    fft2048_regs<-1>(v, bufA, bufB, tw, j);
#pragma unroll
    for (int r = 0; r < 8; ++r) v[r] = cmul(v[r], make_float2(wre[r], wim[r]));
    __syncthreads();
    fft2048_regs<1>(v, bufA, bufB, tw, j);
#pragma unroll
    for (int r = 0; r < 8; ++r) col[j + 256 * r] = v[r];
}

__global__ __launch_bounds__(320) void k_pass3(const float2* __restrict__ Z2T, const bf16* __restrict__ x,
                                               const float* __restrict__ mod, bf16* __restrict__ x1,
                                               bf16* __restrict__ h2) {
    __shared__ float2 buf0[4][D_];
    __shared__ float2 buf1[4][D_];
    __shared__ float2 tw[D_];
    int tid = threadIdx.x;
    int s0 = blockIdx.x * 8;
    for (int m = tid; m < D_; m += 320) {
        float sn, cs; sincospif(-2.f * (float)m / (float)D_, &sn, &cs);
        tw[m] = make_float2(cs, sn);
    }
    for (int task = tid; task < DF_ * 4; task += 320) {
        int p = task & 3, k = task >> 2;
        float4 q = *reinterpret_cast<const float4*>(Z2T + (size_t)k * S_ + s0 + 2 * p);
        if (k == 0 || k == 288) {
            buf0[p][k] = make_float2(q.x, q.z);
        } else {
            buf0[p][k] = make_float2(q.x - q.w, q.y + q.z);
            buf0[p][D_ - k] = make_float2(q.x + q.w, q.z - q.y);
        }
    }
    __syncthreads();
    int w, t;
    if (tid < 256) { w = tid >> 6; t = tid & 63; } else { w = (tid - 256) >> 3; t = 64 + ((tid - 256) & 7); }
    bool act = tid < 288;
    if (act) {
        cf v[8];
#pragma unroll
        for (int r = 0; r < 8; ++r) v[r] = buf0[w][t + 72 * r];
        dft8<1>(v);
#pragma unroll
        for (int r = 0; r < 8; ++r) buf1[w][8 * t + r] = v[r];
    }
    __syncthreads();
    if (act) {
        cf v[8];
        int k = t & 7;
#pragma unroll
        for (int r = 0; r < 8; ++r) v[r] = buf1[w][t + 72 * r];
#pragma unroll
        for (int r = 1; r < 8; ++r) v[r] = cmulc(v[r], tw[r * k * 9]);
        dft8<1>(v);
        int o = (t >> 3) * 64 + k;
#pragma unroll
        for (int r = 0; r < 8; ++r) buf0[w][o + 8 * r] = v[r];
    }
    __syncthreads();
    if (tid < 256) {
        int j = tid & 63;
        cf v[9];
#pragma unroll
        for (int r = 0; r < 9; ++r) v[r] = buf0[w][j + 64 * r];
#pragma unroll
        for (int r = 1; r < 9; ++r) v[r] = cmulc(v[r], tw[(r * j) % D_]);
        dft9<1>(v);
        const float scl = 1.f / ((float)S_ * (float)D_);
        int ra = s0 + 2 * w, rb = ra + 1;
        float xa[9], xb[9];
#pragma unroll
        for (int r = 0; r < 9; ++r) {
            int n = j + 64 * r;
            float g = mod[2 * D_ + n];
            float ya = rbf(v[r].x * scl), yb = rbf(v[r].y * scl);
            xa[r] = rbf(bf2f(x[(size_t)ra * D_ + n]) + rbf(g * ya));
            xb[r] = rbf(bf2f(x[(size_t)rb * D_ + n]) + rbf(g * yb));
        }
        float mba, dena, mbb, denb;
        ln_stats(xa, mba, dena);
        ln_stats(xb, mbb, denb);
#pragma unroll
        for (int r = 0; r < 9; ++r) {
            int n = j + 64 * r;
            float sh = mod[3 * D_ + n], sc1 = rbf(1.f + mod[4 * D_ + n]);
            x1[(size_t)ra * D_ + n] = f2bf(xa[r]);
            x1[(size_t)rb * D_ + n] = f2bf(xb[r]);
            h2[(size_t)ra * D_ + n] = f2bf(ln_apply(xa[r], mba, dena, sc1, sh));
            h2[(size_t)rb * D_ + n] = f2bf(ln_apply(xb[r], mbb, denb, sc1, sh));
        }
    }
}

typedef __attribute__((ext_vector_type(8))) short bf16x8;
typedef __attribute__((ext_vector_type(4))) short s16x4;
typedef __attribute__((ext_vector_type(4))) float f32x4;
typedef __attribute__((address_space(3))) s16x4* lds_s16x4_ptr;
constexpr int G_ASZ = 256 * 64 * 2, G_BSZ = 64 * 144 * 2, G_STAGE = G_ASZ + G_BSZ, G_LDS = G_STAGE * 3;

__device__ __forceinline__ s16x4 lds_tr_read(const char* p) {
    return __builtin_amdgcn_ds_read_tr16_b64_v4i16((lds_s16x4_ptr)(p));
}
__device__ __forceinline__ void glds16(const void* gsrc, unsigned lds_dst) {
    unsigned keep;
    asm volatile("s_mov_b32 %0, m0\n\ts_mov_b32 m0, %2\n\ts_nop 0\n\tglobal_load_lds_dwordx4 %1, off\n\ts_mov_b32 m0, %0"
                 : "=&s"(keep) : "v"(gsrc), "s"(lds_dst) : "memory");
}
template <int LDA, int LDB>
__device__ __forceinline__ void g_issue(const bf16* __restrict__ A, const bf16* __restrict__ B, char* stage,
                                        int kt, int lw, int lane) {
#pragma unroll
    for (int i = 0; i < 8; ++i) {
        int ins = lw * 8 + i;
        int row = ins * 8 + (lane >> 3);
        int c = (lane & 7) ^ ((row >> 1) & 7);
        const bf16* src = A + (size_t)row * LDA + kt * 64 + c * 8;
        glds16(src, __builtin_amdgcn_readfirstlane((unsigned)(size_t)stage + ins * 1024));
    }
    int nb = (lw < 2) ? 5 : 4;
    int b0 = (lw < 2) ? lw * 5 : 10 + (lw - 2) * 4;
#pragma unroll
    for (int i = 0; i < 5; ++i) {
        if (i < nb) {
            int ins = b0 + i;
            int off = ins * 1024 + lane * 16;
            int row = off / 288, cb = off % 288;
            const bf16* src = B + (size_t)(kt * 64 + row) * LDB + (cb >> 1);
            glds16(src, __builtin_amdgcn_readfirstlane((unsigned)(size_t)stage + G_ASZ + ins * 1024));
        }
    }
}

template <int MODE, int LDA, int LDB>
__global__ __launch_bounds__(768, 3) void k_gemm(const bf16* __restrict__ Ag, const bf16* __restrict__ Bg,
                                                 bf16* __restrict__ C, float* __restrict__ P) {
    extern __shared__ __attribute__((aligned(16))) char smem[];
    constexpr int NT = 9;
    int tid = threadIdx.x;
    int lane = tid & 63;
    int w = __builtin_amdgcn_readfirstlane(tid >> 6);
    int bid = blockIdx.x;
    int xcd = bid & 7, idx = bid >> 3;
    int brow, bcol, k0;
    if (MODE == 0) { int bn = xcd * 4 + (idx & 3), bm = idx >> 2; brow = bm * 256; bcol = bn * 144; k0 = 0; }
    else { int bn = idx & 3, bm = idx >> 2; brow = bm * 256; bcol = bn * 144; k0 = xcd * 576; }
    const bf16* A = Ag + (size_t)brow * LDA + k0;
    const bf16* B = Bg + (size_t)k0 * LDB + bcol;

    if (w >= 8) {
        int lw = w - 8;
        g_issue<LDA, LDB>(A, B, smem + 0 * G_STAGE, 0, lw, lane);
        g_issue<LDA, LDB>(A, B, smem + 1 * G_STAGE, 1, lw, lane);
        for (int t = 0; t < NT; ++t) {
            if (t + 1 < NT) {
                if (lw < 2) asm volatile("s_waitcnt vmcnt(13)" ::: "memory");
                else asm volatile("s_waitcnt vmcnt(12)" ::: "memory");
            } else {
                asm volatile("s_waitcnt vmcnt(0)" ::: "memory");
            }
            __builtin_amdgcn_s_barrier();
            if (t + 2 < NT) g_issue<LDA, LDB>(A, B, smem + ((t + 2) % 3) * G_STAGE, t + 2, lw, lane);
        }
        return;
    }
    f32x4 acc[2][9];
#pragma unroll
    for (int i = 0; i < 2; ++i)
#pragma unroll
        for (int j = 0; j < 9; ++j) acc[i][j] = (f32x4){0.f, 0.f, 0.f, 0.f};
    int g = lane >> 4, fr = lane & 15, q = fr >> 2, p = lane & 3;
    int a_off[2][2][2];
#pragma unroll
    for (int sl = 0; sl < 2; ++sl)
#pragma unroll
        for (int mi = 0; mi < 2; ++mi) {
            int row = w * 32 + mi * 16 + fr;
            int c = sl * 4 + (g >> 1);
            int sw = (row >> 1) & 7;
            a_off[sl][mi][0] = row * 128 + ((c ^ sw) << 4) + (g & 1) * 8;
            a_off[sl][mi][1] = row * 128 + (((c + 2) ^ sw) << 4) + (g & 1) * 8;
        }
    int b_off = G_ASZ + (4 * g + q) * 288 + p * 8;
    for (int t = 0; t < NT; ++t) {
        __builtin_amdgcn_s_barrier();
        const char* st = smem + (t % 3) * G_STAGE;
#pragma unroll
        for (int sl = 0; sl < 2; ++sl) {
            bf16x8 af[2], bfr[9];
#pragma unroll
            for (int mi = 0; mi < 2; ++mi) {
                s16x4 lo = *reinterpret_cast<const s16x4*>(st + a_off[sl][mi][0]);
                s16x4 hi = *reinterpret_cast<const s16x4*>(st + a_off[sl][mi][1]);
                af[mi] = __builtin_shufflevector(lo, hi, 0, 1, 2, 3, 4, 5, 6, 7);
            }
#pragma unroll
            for (int ni = 0; ni < 9; ++ni) {
                s16x4 lo = lds_tr_read(st + b_off + sl * 32 * 288 + ni * 32);
                s16x4 hi = lds_tr_read(st + b_off + sl * 32 * 288 + ni * 32 + 16 * 288);
                bfr[ni] = __builtin_shufflevector(lo, hi, 0, 1, 2, 3, 4, 5, 6, 7);
            }
            __builtin_amdgcn_sched_barrier(0);
#pragma unroll
            for (int ni = 0; ni < 9; ++ni)
#pragma unroll
                for (int mi = 0; mi < 2; ++mi)
                    acc[mi][ni] = __builtin_amdgcn_mfma_f32_16x16x32_bf16(bfr[ni], af[mi], acc[mi][ni], 0, 0, 0);
            __builtin_amdgcn_sched_barrier(0);
        }
    }
    if (MODE == 0) {
        char* stg = smem + w * 9216;
#pragma unroll
        for (int mi = 0; mi < 2; ++mi)
#pragma unroll
            for (int ni = 0; ni < 9; ++ni) {
                s16x4 pk;
#pragma unroll
                for (int r = 0; r < 4; ++r) {
                    float hv = rbf(acc[mi][ni][r]);
                    float sv = hv * __builtin_amdgcn_rcpf(1.f + __expf(-hv));
                    pk[r] = __builtin_bit_cast(short, f2bf(sv));
                }
                *reinterpret_cast<s16x4*>(stg + (mi * 16 + fr) * 288 + (ni * 16 + 4 * g) * 2) = pk;
            }
#pragma unroll
        for (int i = 0; i < 9; ++i) {
            int ch = lane + 64 * i;
            int row = ch / 18, cc = ch % 18;
            uint4 v = *reinterpret_cast<const uint4*>(stg + row * 288 + cc * 16);
            *reinterpret_cast<uint4*>(C + (size_t)(brow + w * 32 + row) * LDB + bcol + cc * 8) = v;
        }
    } else {
        float* Pp = P + (size_t)xcd * S_ * D_;
#pragma unroll
        for (int mi = 0; mi < 2; ++mi)
#pragma unroll
            for (int ni = 0; ni < 9; ++ni)
                *reinterpret_cast<f32x4*>(Pp + (size_t)(brow + w * 32 + mi * 16 + fr) * LDB + bcol + ni * 16 + 4 * g) = acc[mi][ni];
    }
}

__global__ __launch_bounds__(256) void k_final(const float* __restrict__ P, const bf16* __restrict__ x1,
                                               const float* __restrict__ gb, bf16* __restrict__ out) {
    int i = (blockIdx.x * 256 + threadIdx.x) * 4;
    float4 a = *reinterpret_cast<const float4*>(P + i);
#pragma unroll
    for (int ks = 1; ks < 8; ++ks) {
        float4 b = *reinterpret_cast<const float4*>(P + (size_t)ks * S_ * D_ + i);
        a.x += b.x; a.y += b.y; a.z += b.z; a.w += b.w;
    }
    int n = i % D_;
    float v[4] = {a.x, a.y, a.z, a.w};
    s16x4 xv = *reinterpret_cast<const s16x4*>(x1 + i);
    s16x4 o;
#pragma unroll
    for (int r = 0; r < 4; ++r) {
        float t = rbf(gb[n + r] * rbf(v[r]));
        float xf = __uint_as_float(((unsigned)(unsigned short)xv[r]) << 16);
        o[r] = __builtin_bit_cast(short, f2bf(xf + t));
    }
    *reinterpret_cast<s16x4*>(out + i) = o;
}

extern "C" void kernel_launch(void* const* d_in, const int* in_sizes, int n_in,
                              void* d_out, int out_size, void* d_ws, size_t ws_size,
                              hipStream_t stream) {
    const bf16* x = (const bf16*)d_in[0];
    const bf16* wr = (const bf16*)d_in[1];
    const bf16* wi = (const bf16*)d_in[2];
    const bf16* w1 = (const bf16*)d_in[4];
    const bf16* w2 = (const bf16*)d_in[5];
    const bf16* c = (const bf16*)d_in[6];
    const bf16* wmod = (const bf16*)d_in[7];
    bf16* out = (bf16*)d_out;
    char* ws = (char*)d_ws;
    float* mod = (float*)ws; ws += 16384;
    float2* tw2048 = (float2*)ws; ws += 16384;
    float2* ZT = (float2*)ws; ws += (size_t)DF_ * S_ * 8 + 4096;
    bf16* x1 = (bf16*)ws; ws += (size_t)S_ * D_ * 2;
    bf16* h2 = (bf16*)ws; ws += (size_t)S_ * D_ * 2;
    bf16* hid = (bf16*)ws; ws += (size_t)S_ * F_ * 2;
    float* P = (float*)ws; ws += (size_t)8 * S_ * D_ * 4;
    unsigned* WT = (unsigned*)ws; ws += (size_t)DF_ * S_ * 4;

    hipFuncSetAttribute((const void*)k_gemm<0, D_, F_>, hipFuncAttributeMaxDynamicSharedMemorySize, G_LDS);
    hipFuncSetAttribute((const void*)k_gemm<1, F_, D_>, hipFuncAttributeMaxDynamicSharedMemorySize, G_LDS);
    k_mod<<<180, 256, 0, stream>>>(c, wmod, mod, tw2048, wr, wi, WT);
    k_pass1<<<S_ / 8, 320, 0, stream>>>(x, mod, ZT);
    k_pass2<<<DF_, 256, 0, stream>>>(ZT, WT, tw2048);
    k_pass3<<<S_ / 8, 320, 0, stream>>>(ZT, x, mod, x1, h2);
    k_gemm<0, D_, F_><<<256, 768, G_LDS, stream>>>(h2, w1, hid, nullptr);
    k_gemm<1, F_, D_><<<256, 768, G_LDS, stream>>>(hid, w2, nullptr, P);
    k_final<<<S_ * D_ / 1024, 256, 0, stream>>>(P, x1, mod + 5 * D_, out);
}
```
